```python
import torch

def _softmax(x, axis=-1):
    return torch.softmax(x, dim=axis)
def _gelu(x):
    return torch.nn.functional.gelu(x, approximate="tanh")
def _silu(x):
    return torch.nn.functional.silu(x)
def _sigmoid(x):
    return torch.sigmoid(x)
def _layernorm(x, axis=-1, eps=1e-5):
    m = x.mean(dim=axis, keepdim=True)
    v = x.var(dim=axis, keepdim=True, unbiased=False)
    return (x - m) / torch.sqrt(v + eps)
def _scatter_add(tgt, idx, src, axis=0):
    return tgt.clone().index_add_(axis, idx, src)
def _segment_sum(src, seg, n):
    out = torch.zeros((n,) + src.shape[1:], dtype=src.dtype)
    return out.index_add_(0, seg, src)
def _pad(x, pw):
    flat = tuple(v for pair in reversed(pw) for v in pair)
    return torch.nn.functional.pad(x, flat)

kernel_name = 'standalone__dense_activation_squared_relu__sampling'

def setup_inputs():

    _w = (lambda a: (lambda t: t.to(torch.bfloat16) if t.is_floating_point() else t)(torch.as_tensor(a)))
    B, S, D, F = 32, 16, 576, 4608
    return {
        "x": _w((lambda *s: torch.randn(*s, dtype=torch.bfloat16) * 0.05)(B, S, D)),
        "w1": _w((lambda *s: torch.randn(*s, dtype=torch.bfloat16) * 0.05)(D, F)),
        "b1": _w((lambda *s: torch.randn(*s, dtype=torch.bfloat16) * 0.05)(F)),
        "w2": _w((lambda *s: torch.randn(*s, dtype=torch.bfloat16) * 0.05)(F, D)),
        "b2": _w((lambda *s: torch.randn(*s, dtype=torch.bfloat16) * 0.05)(D)),
    }

def reference(x, w1, b1, w2, b2):
    h = torch.einsum("bsd,df->bsf", x, w1) + b1
    zero = torch.zeros_like(h)
    r = torch.where(h > zero, h, zero)
    h = r * r
    out = torch.einsum("bsf,fd->bsd", h, w2) + b2
    return out
```

```cpp
#include <hip/hip_runtime.h>
#include <hip/hip_bf16.h>
#include <stdint.h>

typedef __attribute__((ext_vector_type(4))) short v4s;
typedef __attribute__((ext_vector_type(8))) short v8s;
typedef __attribute__((ext_vector_type(8))) __bf16 v8bf;
typedef __attribute__((ext_vector_type(2))) __bf16 v2bf;
typedef __attribute__((ext_vector_type(4))) float v4f;
typedef __attribute__((ext_vector_type(2))) float v2f;
typedef __attribute__((ext_vector_type(4))) unsigned v4u;
typedef __attribute__((ext_vector_type(2))) unsigned v2u;
typedef __attribute__((address_space(3))) char* ldsp;
typedef __attribute__((address_space(1))) unsigned long long gu64;

#define DI __device__ __forceinline__

constexpr int MROWS = 512, DM = 576, FF = 4608;
constexpr int WB0 = 0, WBS = 18432;
constexpr int XB0 = 110592, XBS = 8192;
constexpr int BIAS1 = 159744, BIAS2 = 160256;
constexpr int LDS_BYTES = 160768;
constexpr int WS_EPOCH = 0;
constexpr int WS_FLAG1 = 4096;
constexpr int WS_FLAG2 = WS_FLAG1 + 32768;
constexpr int WS_H = 131072;
constexpr int WS_P = WS_H + 256 * 18432;
constexpr int WS_END = WS_P + 256 * 36864;

constexpr int w2loc(int q) {
  return q == 0 ? WB0 + 3 * WBS : q == 1 ? WB0 + 4 * WBS : q == 2 ? WB0 + 5 * WBS : q == 3 ? XB0 + 3 * XBS
       : q == 4 ? WB0 : q == 5 ? WB0 + WBS : q == 6 ? WB0 + 2 * WBS : q == 7 ? XB0 : WB0 + 3 * WBS;
}
constexpr int grp_addr(int G) { return w2loc(G >> 2) + (G & 3) * 4608; }

template <int N> DI void wait_vm() { asm volatile("s_waitcnt vmcnt(%0)" ::"n"(N) : "memory"); }
DI void barrier() { asm volatile("s_waitcnt lgkmcnt(0)\n\ts_barrier" ::: "memory"); }

DI void dma_group(__amdgpu_buffer_rsrc_t rs, unsigned lds, unsigned soff, const unsigned (&vo)[5]) {
  asm volatile(
      "s_mov_b32 m0, %0\n\t"
      "s_mov_b32 exec_hi, 0\n\t"
      "buffer_load_dwordx4 %3, %1, %2 offen lds\n\t"
      "s_mov_b32 exec_hi, -1\n\t"
      "buffer_load_dwordx4 %4, %1, %2 offen offset:512 lds\n\t"
      "buffer_load_dwordx4 %5, %1, %2 offen offset:1536 lds\n\t"
      "buffer_load_dwordx4 %6, %1, %2 offen offset:2560 lds\n\t"
      "buffer_load_dwordx4 %7, %1, %2 offen offset:3584 lds"
      ::"s"(lds), "s"(rs), "s"(soff), "v"(vo[0]), "v"(vo[1]), "v"(vo[2]), "v"(vo[3]), "v"(vo[4])
      : "memory");
}
DI void dma_x(__amdgpu_buffer_rsrc_t rs, unsigned lds, unsigned soff, unsigned v0, unsigned v1) {
  asm volatile(
      "s_mov_b32 m0, %0\n\t"
      "s_nop 0\n\t"
      "buffer_load_dwordx4 %3, %1, %2 offen lds\n\t"
      "buffer_load_dwordx4 %4, %1, %2 offen offset:1024 lds"
      ::"s"(lds), "s"(rs), "s"(soff), "v"(v0), "v"(v1)
      : "memory");
}
DI void dma_bias(__amdgpu_buffer_rsrc_t rs, unsigned lds, unsigned soff, unsigned v0) {
  asm volatile(
      "s_nop 4\n\t"
      "s_mov_b32 m0, %0\n\t"
      "s_mov_b32 exec_hi, 0\n\t"
      "s_mov_b32 exec_lo, 0x3ffff\n\t"
      "buffer_load_dwordx4 %3, %1, %2 offen lds\n\t"
      "s_mov_b64 exec, -1"
      ::"s"(lds), "s"(rs), "s"(soff), "v"(v0)
      : "memory");
}

DI v8bf rd_a(const ldsp (&vb)[3], int lo, int hi) {
  v4s a = __builtin_amdgcn_ds_read_tr16_b64_v4i16((__attribute__((address_space(3))) v4s*)(vb[lo >> 16] + (lo & 65535)));
  v4s b = __builtin_amdgcn_ds_read_tr16_b64_v4i16((__attribute__((address_space(3))) v4s*)(vb[hi >> 16] + (hi & 65535)));
  v8s ab = __builtin_shufflevector(a, b, 0, 1, 2, 3, 4, 5, 6, 7);
  return __builtin_bit_cast(v8bf, ab);
}
DI unsigned pk_bf16(float a, float b) {
  v2f f = {a, b};
  return __builtin_bit_cast(unsigned, __builtin_convertvector(f, v2bf));
}
DI float bf_lo(unsigned u) { return __builtin_bit_cast(float, u << 16); }
DI float bf_hi(unsigned u) { return __builtin_bit_cast(float, u & 0xffff0000u); }
DI v4f mfma(v8bf a, v8bf b, v4f c) { return __builtin_amdgcn_mfma_f32_16x16x32_bf16(a, b, c, 0, 0, 0); }

#define SCHED() __builtin_amdgcn_sched_barrier(0)
#define SG_MFMA(n) __builtin_amdgcn_sched_group_barrier(0x008, n, 0)
#define SG_DSR(n) __builtin_amdgcn_sched_group_barrier(0x100, n, 0)
DI void poll_flags(gu64* p, unsigned long long want) {
#pragma nounroll
  for (int spins = 0; spins < 200000; ++spins) {
    unsigned long long v = __hip_atomic_load(p, __ATOMIC_RELAXED, __HIP_MEMORY_SCOPE_AGENT);
    if (__all(v == want)) break;
    __builtin_amdgcn_s_sleep(1);
  }
  __builtin_amdgcn_fence(__ATOMIC_ACQUIRE, "wavefront");
  asm volatile("" ::: "memory");
}

#define STAMP(i)
#define STAMP_OUT()

constexpr int PRE = 8;
constexpr int ks_lo(int s) { return s < 4 ? 9 + 2 * s : s == 4 ? 8 : s < 9 ? 18 + 2 * (s - 5) : s < 13 ? 27 + 2 * (s - 9) : 26; }
constexpr int ks_hi(int s) { return s == 4 ? 17 : s == 13 ? 35 : ks_lo(s) + 1; }

__global__ __launch_bounds__(256, 1) __attribute__((amdgpu_waves_per_eu(1, 1))) void mlp_fused(const char* __restrict__ x, const char* __restrict__ w1,
                                                    const char* __restrict__ b1, const char* __restrict__ w2,
                                                    const char* __restrict__ b2, char* __restrict__ out,
                                                    char* __restrict__ ws) {
  extern __shared__ __attribute__((aligned(16))) char smem_[];
  const ldsp smem = (ldsp)smem_;
  const unsigned lds0 = (unsigned)(uintptr_t)smem;
  const int b = blockIdx.x, j = b & 7, mt = b >> 5, c = (b >> 3) & 3;
  const int lane = threadIdx.x & 63;
  const int wave = __builtin_amdgcn_readfirstlane(threadIdx.x >> 6);
  STAMP(0);

  const __amdgpu_buffer_rsrc_t rx = __builtin_amdgcn_make_buffer_rsrc((void*)x, 0, MROWS * DM * 2, 0x00020000);
  const __amdgpu_buffer_rsrc_t rw1 = __builtin_amdgcn_make_buffer_rsrc((void*)w1, 0, DM * FF * 2, 0x00020000);
  const __amdgpu_buffer_rsrc_t rw2 = __builtin_amdgcn_make_buffer_rsrc((void*)w2, 0, DM * FF * 2, 0x00020000);
  const __amdgpu_buffer_rsrc_t rws = __builtin_amdgcn_make_buffer_rsrc((void*)ws, 0, WS_END, 0x00020000);

  unsigned vo1[5], vo2[5];
#pragma unroll
  for (int p = 0; p < 5; ++p) {
    const int q = p == 0 ? (lane & 31) : 32 + 64 * (p - 1) + lane;
    const int rr = q / 18, cc = q % 18;
    const int imm = p == 0 ? 0 : 512 + 1024 * (p - 1);
    vo1[p] = (32 * (wave >> 1) + 8 * (rr >> 2) + 4 * (wave & 1) + (rr & 3)) * (FF * 2) + cc * 16 - imm;
    vo2[p] = rr * (DM * 2) + cc * 16 - imm;
  }
  const unsigned vx0 = (mt * 64 + 16 * wave + (lane >> 3)) * (DM * 2) + (((lane & 7) ^ ((lane >> 3) & 6)) * 16);
  const unsigned vx1 = vx0 + 8 * DM * 2 - 1024;
  const unsigned so1 = (j * 576 + c * 144) * 2;

  {
    const bool odd = wave & 1;
    const __amdgpu_buffer_rsrc_t rb =
        __builtin_amdgcn_make_buffer_rsrc((void*)(odd ? b2 : b1), 0, odd ? DM * 2 : FF * 2, 0x00020000);
    dma_bias(rb, lds0 + (odd ? BIAS2 : BIAS1), odd ? c * 288 : so1, lane * 16);
  }
  auto stage1 = [&](int st, int buf) {
    dma_x(rx, lds0 + XB0 + buf * XBS + wave * 2048, st * 128, vx0, vx1);
    dma_group(rw1, lds0 + WB0 + buf * WBS + wave * 4608, so1 + st * 64 * FF * 2, vo1);
  };
  auto stage2 = [&](int q) {
    const int G = 4 * q + wave;
    const int pp = G / 9, i = G - 9 * pp;
    const unsigned so = (j * 576 + ((pp + c) & 3) * 144 + i * 16) * (DM * 2) + c * 288;
    dma_group(rw2, lds0 + w2loc(q) + wave * 4608, so, vo2);
  };
#pragma unroll
  for (int st = 0; st < 6; ++st) stage1(st, st);

  gu64* const epoch_p = (gu64*)(ws + WS_EPOCH + mt * 256);
  unsigned long long want = __hip_atomic_load(epoch_p, __ATOMIC_RELAXED, __HIP_MEMORY_SCOPE_AGENT);

  ldsp vb[3];
  vb[0] = smem + (lane >> 2) * 288 + (lane & 3) * 8;
  vb[1] = vb[0] + 65536;
  vb[2] = vb[0] + 131072;
  asm volatile("" : "+v"(vb[1]), "+v"(vb[2]));
  const int fi = lane & 15, fg = lane >> 4;
  const ldsp xb0 = smem + XB0 + wave * 2048 + (fi >> 3) * 1024 + (fi & 7) * 128 + ((fg ^ (fi & 6)) * 16);
  const ldsp xb1 = smem + XB0 + wave * 2048 + (fi >> 3) * 1024 + (fi & 7) * 128 + (((4 + fg) ^ (fi & 6)) * 16);

  v4f acc[9];
#pragma unroll
  for (int t = 0; t < 9; ++t) acc[t] = v4f{0.f, 0.f, 0.f, 0.f};

  auto compute1 = [&](int buf) {
    v8bf F0[9], F1[9];
    const v8bf xa = *(__attribute__((address_space(3))) v8bf*)(xb0 + buf * XBS);
#pragma unroll
    for (int t = 0; t < 9; ++t) { const int o = WB0 + buf * WBS + t * 32; F0[t] = rd_a(vb, o, o + 4608); }
    const v8bf xc = *(__attribute__((address_space(3))) v8bf*)(xb1 + buf * XBS);
#pragma unroll
    for (int t = 0; t < 9; ++t) { const int o = WB0 + buf * WBS + 9216 + t * 32; F1[t] = rd_a(vb, o, o + 4608); }
#pragma unroll
    for (int t = 0; t < 9; ++t) acc[t] = mfma(F0[t], xa, acc[t]);
#pragma unroll
    for (int t = 0; t < 9; ++t) acc[t] = mfma(F1[t], xc, acc[t]);
    SG_DSR(14);
#pragma unroll
    for (int t = 0; t < 12; ++t) { SG_MFMA(1); SG_DSR(2); }
    SG_MFMA(6);
    SCHED();
  };
  wait_vm<36>(); barrier(); STAMP(1);                          compute1(0);
  wait_vm<29>(); barrier(); stage1(6, 0);                      compute1(1);
  wait_vm<29>(); barrier(); stage1(7, 1);                      compute1(2);
  wait_vm<29>(); barrier(); stage1(8, 2);                      compute1(3);
  wait_vm<29>(); barrier(); stage2(0);                         compute1(4);
  wait_vm<27>(); barrier(); stage2(1);                         compute1(5);
  wait_vm<24>(); barrier(); stage2(2); stage2(3);              compute1(0);
  wait_vm<27>(); barrier(); stage2(4);                         compute1(1);
  wait_vm<25>(); barrier(); STAMP(2); stage2(5);               compute1(2);

  v2u hp[9];
#pragma unroll
  for (int t = 0; t < 9; ++t) {
    const v2u bb = *(__attribute__((address_space(3))) v2u*)(smem + BIAS1 + t * 32 + fg * 8);
    const unsigned a0 = pk_bf16(acc[t][0], acc[t][1]), a1 = pk_bf16(acc[t][2], acc[t][3]);
    const unsigned s0 = pk_bf16(fmaxf(bf_lo(a0) + bf_lo(bb[0]), 0.f), fmaxf(bf_hi(a0) + bf_hi(bb[0]), 0.f));
    const unsigned s1 = pk_bf16(fmaxf(bf_lo(a1) + bf_lo(bb[1]), 0.f), fmaxf(bf_hi(a1) + bf_hi(bb[1]), 0.f));
    hp[t][0] = pk_bf16(bf_lo(s0) * bf_lo(s0), bf_hi(s0) * bf_hi(s0));
    hp[t][1] = pk_bf16(bf_lo(s1) * bf_lo(s1), bf_hi(s1) * bf_hi(s1));
  }
  {
    const unsigned ho = WS_H + b * 18432 + wave * 4608;
#pragma unroll
    for (int s = 0; s < 4; ++s) {
      const v4u v = {hp[2 * s][0], hp[2 * s][1], hp[2 * s + 1][0], hp[2 * s + 1][1]};
      __builtin_amdgcn_raw_buffer_store_b128(v, rws, lane * 16 + s * 1024, ho, 16);
    }
    __builtin_amdgcn_raw_buffer_store_b64(hp[8], rws, lane * 8 + 4096, ho, 16);
  }
  STAMP(3);
  wait_vm<25>(); barrier();
  stage2(6); stage2(7);
#pragma unroll
  for (int t = 0; t < 9; ++t) acc[t] = v4f{0.f, 0.f, 0.f, 0.f};
  {
    v8bf F[4][9];
#pragma unroll
    for (int s = 0; s < 4; ++s)
#pragma unroll
      for (int t = 0; t < 9; ++t) F[s][t] = rd_a(vb, grp_addr(2 * s) + t * 32, grp_addr(2 * s + 1) + t * 32);
#pragma unroll
    for (int s = 0; s < 4; ++s) {
      const v4u v = {hp[2 * s][0], hp[2 * s][1], hp[2 * s + 1][0], hp[2 * s + 1][1]};
      const v8bf hb = __builtin_bit_cast(v8bf, v);
#pragma unroll
      for (int t = 0; t < 9; ++t) acc[t] = mfma(F[s][t], hb, acc[t]);
    }
    SG_DSR(14);
#pragma unroll
    for (int t = 0; t < 29; ++t) { SG_MFMA(1); SG_DSR(2); }
    SG_MFMA(7);
    SCHED();
  }
  wait_vm<10>(); barrier();
  asm volatile("" : "+v"(want));
  want += 1ull;
  if (threadIdx.x == 0)
    __hip_atomic_store((gu64*)(ws + WS_FLAG1 + b * 128), want, __ATOMIC_RELAXED, __HIP_MEMORY_SCOPE_AGENT);
  STAMP(4);
  stage2(8);
  v8bf A[PRE][9];
#pragma unroll
  for (int s = 0; s < PRE / 2; ++s)
#pragma unroll
    for (int t = 0; t < 9; ++t) A[s][t] = rd_a(vb, grp_addr(ks_lo(s)) + t * 32, grp_addr(ks_hi(s)) + t * 32);
  asm volatile("" ::: "memory");
  SCHED();
  {
    const int pp = 1 + (lane % 3);
    const int pb = (b & ~24) | (((c + pp) & 3) << 3);
    poll_flags((gu64*)(ws + WS_FLAG1 + pb * 128), want);
  }
  STAMP(5);
  v8bf B[14];
  {
    v2u l8[3];
#pragma unroll
    for (int pp = 1; pp < 4; ++pp) {
      const int pb = (b & ~24) | (((c + pp) & 3) << 3);
      const unsigned ho = WS_H + pb * 18432 + wave * 4608;
#pragma unroll
      for (int s = 0; s < 4; ++s) {
        const int ks = (pp == 1 ? 0 : pp == 2 ? 5 : 9) + s;
        B[ks] = __builtin_bit_cast(v8bf, __builtin_amdgcn_raw_buffer_load_b128(rws, lane * 16 + s * 1024, ho, 16));
      }
      l8[pp - 1] = __builtin_amdgcn_raw_buffer_load_b64(rws, lane * 8 + 4096, ho, 16);
    }
    SCHED();
    asm volatile("s_barrier" ::: "memory");
#pragma unroll
    for (int s = PRE / 2; s < PRE; ++s)
#pragma unroll
      for (int t = 0; t < 9; ++t) A[s][t] = rd_a(vb, grp_addr(ks_lo(s)) + t * 32, grp_addr(ks_hi(s)) + t * 32);
    const v4u j0 = {hp[8][0], hp[8][1], l8[0][0], l8[0][1]};
    const v4u j1 = {l8[1][0], l8[1][1], l8[2][0], l8[2][1]};
    B[4] = __builtin_bit_cast(v8bf, j0);
    B[13] = __builtin_bit_cast(v8bf, j1);
  }
  {
    const unsigned po = WS_P + b * 36864 + wave * 9216;
    v8bf J[2][14 - PRE];
#pragma unroll
    for (int s = PRE; s < 14; ++s) J[0][s - PRE] = rd_a(vb, grp_addr(ks_lo(s)), grp_addr(ks_hi(s)));
    SCHED();
#pragma unroll
    for (int t = 0; t < 9; ++t) {
      if (t < 8) {
#pragma unroll
        for (int s = PRE; s < 14; ++s)
          J[(t + 1) & 1][s - PRE] = rd_a(vb, grp_addr(ks_lo(s)) + (t + 1) * 32, grp_addr(ks_hi(s)) + (t + 1) * 32);
      }
#pragma unroll
      for (int s = 0; s < PRE; ++s) acc[t] = mfma(A[s][t], B[s], acc[t]);
#pragma unroll
      for (int s = PRE; s < 14; ++s) acc[t] = mfma(J[t & 1][s - PRE], B[s], acc[t]);
      if (t > 0)
        __builtin_amdgcn_raw_buffer_store_b128(__builtin_bit_cast(v4u, acc[t - 1]), rws, lane * 16 + (t - 1) * 1024, po, 16);
      SG_MFMA(2);
      __builtin_amdgcn_sched_group_barrier(0x040, 1, 0);
#pragma unroll
      for (int s = 0; s < 14 - PRE; ++s) { SG_MFMA(1); SG_DSR(2); }
      SG_MFMA(PRE - 2);
      SCHED();
    }
    __builtin_amdgcn_raw_buffer_store_b128(__builtin_bit_cast(v4u, acc[8]), rws, lane * 16 + 8 * 1024, po, 16);
  }
  STAMP(6);
  wait_vm<0>(); barrier();
  if (threadIdx.x == 0)
    __hip_atomic_store((gu64*)(ws + WS_FLAG2 + b * 128), want, __ATOMIC_RELAXED, __HIP_MEMORY_SCOPE_AGENT);
  STAMP(7);
  {
    int jj = lane & 7;
    if (jj == j) jj = (j + 1) & 7;
    poll_flags((gu64*)(ws + WS_FLAG2 + ((b & ~7) | jj) * 128), want);
  }
  STAMP(8);
  {
    const bool extra = (j == 2 * wave);
    const unsigned v0 = lane * 16 + j * 1024;
    const unsigned v1 = extra ? lane * 16 + 8 * 1024 : 0x80000000u;
    v4f p0[8], p1[8];
#pragma unroll
    for (int jj = 0; jj < 8; ++jj) {
      const unsigned po = WS_P + ((b & ~7) | jj) * 36864 + wave * 9216;
      p0[jj] = __builtin_bit_cast(v4f, __builtin_amdgcn_raw_buffer_load_b128(rws, v0, po, 16));
      p1[jj] = __builtin_bit_cast(v4f, __builtin_amdgcn_raw_buffer_load_b128(rws, v1, po, 16));
    }
    SCHED();
    const v2u c0 = *(__attribute__((address_space(3))) v2u*)(smem + BIAS2 + j * 32 + fg * 8);
    const v2u c1 = *(__attribute__((address_space(3))) v2u*)(smem + BIAS2 + 8 * 32 + fg * 8);
    v4f s0 = p0[0], s1 = p1[0];
#pragma unroll
    for (int jj = 1; jj < 8; ++jj) { s0 += p0[jj]; s1 += p1[jj]; }
    const __amdgpu_buffer_rsrc_t ro = __builtin_amdgcn_make_buffer_rsrc((void*)out, 0, MROWS * DM * 2, 0x00020000);
    const unsigned ob = (mt * 64 + wave * 16 + fi) * (DM * 2) + c * 288 + fg * 8;
    {
      const unsigned a0 = pk_bf16(s0[0], s0[1]), a1 = pk_bf16(s0[2], s0[3]);
      const v2u o = {pk_bf16(bf_lo(a0) + bf_lo(c0[0]), bf_hi(a0) + bf_hi(c0[0])),
                     pk_bf16(bf_lo(a1) + bf_lo(c0[1]), bf_hi(a1) + bf_hi(c0[1]))};
      __builtin_amdgcn_raw_buffer_store_b64(o, ro, ob + j * 32, 0, 0);
    }
    {
      const unsigned a0 = pk_bf16(s1[0], s1[1]), a1 = pk_bf16(s1[2], s1[3]);
      const v2u o = {pk_bf16(bf_lo(a0) + bf_lo(c1[0]), bf_hi(a0) + bf_hi(c1[0])),
                     pk_bf16(bf_lo(a1) + bf_lo(c1[1]), bf_hi(a1) + bf_hi(c1[1]))};
      __builtin_amdgcn_raw_buffer_store_b64(o, ro, extra ? ob + 8 * 32 : 0x80000000u, 0, 0);
    }
  }
  if ((b & 31) == 0 && threadIdx.x == 0)
    __hip_atomic_store(epoch_p, want, __ATOMIC_RELAXED, __HIP_MEMORY_SCOPE_AGENT);
  STAMP(9);
  STAMP_OUT();
}

extern "C" void kernel_launch(void* const* d_in, const int* in_sizes, int n_in, void* d_out, int out_size,
                              void* d_ws, size_t ws_size, hipStream_t stream) {
  (void)in_sizes; (void)n_in; (void)out_size; (void)ws_size;
  hipFuncSetAttribute((const void*)mlp_fused, hipFuncAttributeMaxDynamicSharedMemorySize, LDS_BYTES);
  mlp_fused<<<256, 256, LDS_BYTES, stream>>>((const char*)d_in[0], (const char*)d_in[1], (const char*)d_in[2],
                                             (const char*)d_in[3], (const char*)d_in[4], (char*)d_out, (char*)d_ws);
}
```

```cpp
#include <hip/hip_runtime.h>
#include <hip/hip_bf16.h>
#include <stdint.h>
#include <type_traits>

typedef __attribute__((ext_vector_type(4))) short v4s;
typedef __attribute__((ext_vector_type(8))) short v8s;
typedef __attribute__((ext_vector_type(8))) __bf16 v8bf;
typedef __attribute__((ext_vector_type(2))) __bf16 v2bf;
typedef __attribute__((ext_vector_type(4))) float v4f;
typedef __attribute__((ext_vector_type(2))) float v2f;
typedef __attribute__((ext_vector_type(4))) unsigned v4u;
typedef __attribute__((ext_vector_type(2))) unsigned v2u;
typedef __attribute__((address_space(3))) char* ldsp;
typedef __attribute__((address_space(1))) unsigned long long gu64;
typedef unsigned long long u64;
typedef __amdgpu_buffer_rsrc_t rsrc_t;

#define DI __device__ __forceinline__
#define IC(n) std::integral_constant<int, n>

#define DEPTH 5
#define PSLEEP 2

constexpr int MROWS = 512, DM = 576, FF = 4608;
constexpr int BIAS1 = 147456;
constexpr int LDS_BYTES = 147456 + 1024;
constexpr int WS_EPOCH = 0;
constexpr int WS_H = 4096;
constexpr int WS_P = WS_H + 256 * 24576;
constexpr int WS_END = WS_P + 256 * 24576;
constexpr int SPIN_MAX = 100000;
#define STAMP(i)
#define STAMP_OUT()
#define WB(n) do { wait_vm<n>(); bar(); } while (0)

template <int I, int N, class F> DI void sfor(F&& f) {
  if constexpr (I < N) { f(IC(I){}); sfor<I + 1, N>(f); }
}
template <int N> DI void wait_vm() { asm volatile("s_waitcnt vmcnt(%0)" ::"n"(N) : "memory"); }
DI void bar() { asm volatile("s_barrier" ::: "memory"); }
DI void bar_lgkm() { asm volatile("s_waitcnt lgkmcnt(0)\n\ts_barrier" ::: "memory"); }

DI void dma_unit(rsrc_t rs, unsigned lds, unsigned soff, unsigned v0, unsigned v1) {
  asm volatile(
      "s_mov_b32 m0, %0\n\t"
      "s_nop 0\n\t"
      "buffer_load_dwordx4 %3, %1, %2 offen lds\n\t"
      "buffer_load_dwordx4 %4, %1, %2 offen offset:1024 lds"
      ::"s"(lds), "s"(rs), "s"(soff), "v"(v0), "v"(v1)
      : "memory");
}
DI void dma_one(rsrc_t rs, unsigned lds, unsigned soff, unsigned v0) {
  asm volatile(
      "s_nop 4\n\t"
      "s_mov_b32 m0, %0\n\t"
      "s_nop 0\n\t"
      "buffer_load_dwordx4 %3, %1, %2 offen lds"
      ::"s"(lds), "s"(rs), "s"(soff), "v"(v0)
      : "memory");
}
DI v8bf rd_tr(ldsp p, int lo, int hi) {
  v4s a = __builtin_amdgcn_ds_read_tr16_b64_v4i16((__attribute__((address_space(3))) v4s*)(p + lo));
  v4s b = __builtin_amdgcn_ds_read_tr16_b64_v4i16((__attribute__((address_space(3))) v4s*)(p + hi));
  v8s ab = __builtin_shufflevector(a, b, 0, 1, 2, 3, 4, 5, 6, 7);
  return __builtin_bit_cast(v8bf, ab);
}
DI unsigned pk_bf16(float a, float b) {
  v2f f = {a, b};
  return __builtin_bit_cast(unsigned, __builtin_convertvector(f, v2bf));
}
DI float bf_lo(unsigned u) { return __builtin_bit_cast(float, u << 16); }
DI float bf_hi(unsigned u) { return __builtin_bit_cast(float, u & 0xffff0000u); }
DI v4f mfma(v8bf a, v8bf b, v4f c) { return __builtin_amdgcn_mfma_f32_16x16x32_bf16(a, b, c, 0, 0, 0); }
DI v4u ld16(rsrc_t r, unsigned v, unsigned s) { return __builtin_amdgcn_raw_buffer_load_b128(r, v, s, 16); }
DI void st16(v4u d, rsrc_t r, unsigned v, unsigned s) { __builtin_amdgcn_raw_buffer_store_b128(d, r, v, s, 16); }
typedef __attribute__((address_space(1))) unsigned gu32;
DI unsigned ld_flag(const char* p) { return __hip_atomic_load((gu32*)p, __ATOMIC_RELAXED, __HIP_MEMORY_SCOPE_AGENT); }
DI void st_flag(char* p, unsigned v) { __hip_atomic_store((gu32*)p, v, __ATOMIC_RELAXED, __HIP_MEMORY_SCOPE_AGENT); }
DI void adj(ldsp& p, int d) { p += d; asm volatile("" : "+v"(p)); }

#define SCHED() __builtin_amdgcn_sched_barrier(0)
#define SG(m, n) __builtin_amdgcn_sched_group_barrier(m, n, 0)

DI bool untag_h(v4u& b0, v4u& b1, unsigned tag) {
  b0 ^= tag; b1 ^= tag;
  const unsigned o = (b0[0] | b0[1] | b0[2]) | (b0[3] | b1[0] | b1[1]) | (b1[2] | b1[3]);
  return __all((o & 0x80008000u) == 0);
}
DI void ensure_h(v4u& b0, v4u& b1, unsigned tag, rsrc_t rws, unsigned v, unsigned s) {
  if (__builtin_expect(!untag_h(b0, b1, tag), 0)) {
#pragma nounroll
    for (int sp = 0; sp < SPIN_MAX; ++sp) {
      asm volatile("" ::: "memory");
      b0 = ld16(rws, v, s);
      b1 = ld16(rws, v + 1024, s);
      if (untag_h(b0, b1, tag)) break;
    }
  }
}

constexpr int u1_addr(int v) { return v < 9 ? v * 16384 + 8192 : ((v - 9) >> 1) * 16384 + ((v - 9) & 1) * 8192; }
template <int NL> constexpr int u2_addr(int q, int line) {
  return NL == 2 ? q * 16384 + line * 8192 : (q % 6) * 24576 + line * 8192;
}

DI void reduce_tile(rsrc_t rws, rsrc_t ro, unsigned pbase, unsigned voff, unsigned ptag, v2u bb, unsigned ooff) {
  v2u p[8];
#pragma nounroll
  for (int sp = 0; sp < SPIN_MAX; ++sp) {
    asm volatile("" ::: "memory");
#pragma unroll
    for (int jj = 0; jj < 8; ++jj) p[jj] = __builtin_amdgcn_raw_buffer_load_b64(rws, voff, pbase + jj * 24576, 16);
    unsigned m = 0;
#pragma unroll
    for (int jj = 0; jj < 8; ++jj) { p[jj] ^= ptag; m |= p[jj][0] | p[jj][1]; }
    if (__all((m & 0x00010001u) == 0)) break;
  }
  v4f s = {bf_lo(bb[0]), bf_hi(bb[0]), bf_lo(bb[1]), bf_hi(bb[1])};
#pragma unroll
  for (int jj = 0; jj < 8; ++jj) {
    const v4u u = {(p[jj][0] << 16) | 0x8000u, (p[jj][0] & 0xffff0000u) | 0x8000u,
                   (p[jj][1] << 16) | 0x8000u, (p[jj][1] & 0xffff0000u) | 0x8000u};
    s += __builtin_bit_cast(v4f, u);
  }
  const v2u o = {pk_bf16(s[0], s[1]), pk_bf16(s[2], s[3])};
  __builtin_amdgcn_raw_buffer_store_b64(o, ro, ooff, 0, 0);
}

__global__ __launch_bounds__(512, 2) void mlp_fused(const char* __restrict__ x, const char* __restrict__ w1,
                                                    const char* __restrict__ b1, const char* __restrict__ w2,
                                                    const char* __restrict__ b2, char* __restrict__ out,
                                                    char* __restrict__ ws) {
  extern __shared__ __attribute__((aligned(16))) char smem_[];
  const ldsp smem = (ldsp)smem_;
  const unsigned lds0 = (unsigned)(uintptr_t)smem;
  const int b = blockIdx.x, j = b & 7, mt = b >> 5, c = (b >> 3) & 3;
  const int lane = threadIdx.x & 63;
  const int wave = __builtin_amdgcn_readfirstlane(threadIdx.x >> 6);
  STAMP(0);
  asm volatile("" ::"s"(x), "s"(w1), "s"(b1), "s"(w2), "s"(b2), "s"(out), "s"(ws));
  char* const epoch_p = ws + WS_EPOCH + mt * 256;
  const unsigned lsel = (unsigned)(0x210210210210ull >> (4 * mt));
  const unsigned l0 = lsel & 3, l1 = (lsel >> 4) & 3, l2 = (lsel >> 8) & 3;
  const bool helper = c == 3;
  unsigned want;

  if (__builtin_expect(wave >= 4, 1)) {
    const int lw = wave - 4, r8 = lane >> 3, p16 = lane & 7;
    const rsrc_t rw2 = __builtin_amdgcn_make_buffer_rsrc((void*)w2, 0, DM * FF * 2, 0x00020000);
    const unsigned mb = lds0 + lw * 2048;
    const unsigned v20 = (16 * lw + r8) * (DM * 2) + ((p16 ^ (r8 & 6)) * 16);
    const unsigned v21 = v20 + 8 * DM * 2 - 1024;
    const unsigned w2s = j * (576 * DM * 2) + c * 256;
    v2u bb;
    int lane2 = lane;
    auto late = [&]() {
      asm volatile("" : "+v"(lane2));
      const rsrc_t rb2 = __builtin_amdgcn_make_buffer_rsrc((void*)b2, 0, DM * 2, 0x00020000);
      bb = __builtin_amdgcn_raw_buffer_load_b64(
          rb2, (128 * c + 64 * (lw >> 1) + 32 * ((j >> 1) & 1) + 16 * (lw & 1) + 4 * (lane2 >> 4)) * 2, 0, 0);
      SCHED();
    };
    if (!helper) {
      const rsrc_t rx = __builtin_amdgcn_make_buffer_rsrc((void*)x, 0, MROWS * DM * 2, 0x00020000);
      const rsrc_t rw1 = __builtin_amdgcn_make_buffer_rsrc((void*)w1, 0, DM * FF * 2, 0x00020000);
      const unsigned cb = (j * 576 + c * 192) * 2;
      const unsigned v10 = (16 * lw + r8) * (FF * 2) + ((p16 ^ (r8 & 2)) * 16);
      const unsigned v11 = (v10 ^ 64) + 8 * FF * 2 - 1024;
      unsigned ko = 3 * ((0x10210u >> (4 * (c + ((mt * 11) >> 5)))) & 3);
      const unsigned xs = mt * 64 * DM * 2;
      asm volatile("s_nop 4");
#pragma nounroll
      for (int t = 0; t < 9; ++t) {
        if (t >= DEPTH) { wait_vm<4 * (DEPTH - 1)>(); bar(); }
        dma_unit(rx, mb + t * 16384, xs + ko * 128, v20, v21);
        dma_unit(rw1, mb + t * 16384 + 8192, ko * (64 * FF * 2) + cb + l0 * 128, v10, v11);
        ko = ko == 8 ? 0 : ko + 1;
        if (t == 0) want = ld_flag(epoch_p);
      }
      STAMP(1);
#pragma nounroll
      for (int t = 0; t < 9; ++t) {
        WB(4 * (DEPTH - 1));
        dma_unit(rw1, mb + t * 16384, ko * (64 * FF * 2) + cb + (t < 5 ? l1 : l2) * 128, v10, v11);
        ko = ko == 8 ? 0 : ko + 1;
        dma_unit(rw1, mb + t * 16384 + 8192, ko * (64 * FF * 2) + cb + (t < 4 ? l1 : l2) * 128, v10, v11);
        ko = ko == 8 ? 0 : ko + 1;
      }
      STAMP(2);
      const unsigned csel = 0x10210u >> (4 * c), a0 = 3 * c, a1 = 3 * ((csel >> 4) & 3), a2 = 3 * ((csel >> 8) & 3);
      u64 tab = (l0 * 0x00011001u + l1 * 0x01100010u + l2 * 0x10000100u + a0 * 0x111u + a1 * 0x10101000u + a2 * 0x01010000u) |
                ((u64)(l2 + a2) << 32);
#pragma nounroll
      for (int q = 0; q < 9; ++q) {
        const unsigned so = w2s + ((unsigned)tab & 15) * (64 * DM * 2);
        tab >>= 4;
        WB(4 * (DEPTH - 1));
        dma_unit(rw2, mb + q * 16384, so, v20, v21);
        dma_unit(rw2, mb + q * 16384 + 8192, so + 128, v20, v21);
      }
      STAMP(3);
      late();
      sfor<0, DEPTH>([&](auto r) { wait_vm<4 * (DEPTH - 1 - r) + 1>(); bar(); });
    } else {
      u64 tab = (l0 * 0x111u + l1 * 0x111000u + l2 * 0x11000000u + 0x30630630u) | ((u64)(l2 + 6) << 32);
      auto issue = [&](int slot) {
        const unsigned so = w2s + ((unsigned)tab & 15) * (64 * DM * 2);
        tab >>= 4;
        dma_unit(rw2, mb + slot * 24576, so, v20, v21);
        dma_unit(rw2, mb + slot * 24576 + 8192, so + 128, v20, v21);
        dma_unit(rw2, mb + slot * 24576 + 16384, so + 256, v20, v21);
      };
      want = ld_flag(epoch_p);
      asm volatile("s_nop 4");
#pragma nounroll
      for (int q = 0; q < 6; ++q) issue(q);
      STAMP(1);
      wait_vm<30>(); bar();
      wait_vm<24>(); bar();
#pragma nounroll
      for (int q = 6; q < 9; ++q) { wait_vm<18>(); bar(); issue(q - 6); }
      STAMP(3);
      late();
      wait_vm<19>(); bar();
      wait_vm<13>(); bar();
      wait_vm<7>(); bar();
      wait_vm<1>(); bar();
    }
    const rsrc_t rws = __builtin_amdgcn_make_buffer_rsrc((void*)ws, 0, WS_END, 0x00020000);
    const rsrc_t ro = __builtin_amdgcn_make_buffer_rsrc((void*)out, 0, MROWS * DM * 2, 0x00020000);
    const int orow = mt * 64 + 32 * (j >> 2) + 16 * (j & 1) + (lane2 & 15);
    const int ocol = 128 * c + 64 * (lw >> 1) + 32 * ((j >> 1) & 1) + 16 * (lw & 1) + 4 * (lane2 >> 4);
    STAMP(4);
    asm volatile("" : "+v"(want));
    want = __builtin_amdgcn_readfirstlane(want) + 1;
    bar();
    STAMP(6);
    bar();
    __builtin_amdgcn_s_sleep(PSLEEP);
    STAMP(7);
    reduce_tile(rws, ro, WS_P + (b & ~7) * 24576 + j * 3072, (lw >> 1) * 1024 + lane2 * 16 + (lw & 1) * 8,
                (want & 1) ? 1u : 0x10000u, bb, orow * (DM * 2) + ocol * 2);
    if (b == (mt * 32 + 24) && threadIdx.x == 256) st_flag(epoch_p, want);
    STAMP(9);
    STAMP_OUT();
    return;
  }

  want = ld_flag(epoch_p);
  const rsrc_t rws = __builtin_amdgcn_make_buffer_rsrc((void*)ws, 0, WS_END, 0x00020000);
  const int fn = lane & 15, fg = lane >> 4;
  const int rh = wave >> 1, ch = wave & 1;
  const unsigned l16 = lane * 16;
  const unsigned pown = WS_P + b * 24576 + wave * 6144;
  unsigned htag;
  ldsp w2a[4];
#pragma unroll
  for (int t = 0; t < 4; ++t)
    w2a[t] = smem + ch * 4096 + (lane >> 2) * 128 + ((t ^ (2 * (ch ^ 1)) ^ ((lane >> 3) & 3)) * 32) + (lane & 3) * 8;

  auto phase2 = [&](auto nl, v4u (&B)[9][2], auto&& pre, auto&& hook) {
    constexpr int NL = nl;
    constexpr int NZ = 9 * 2 * NL;
    v4f acc[2][NL][4];
    v8bf A[3][2];
    auto reads = [&](auto z_) {
      constexpr int z = z_, q = z / (2 * NL), i = z % (2 * NL), half = i / NL, line = i % NL;
      constexpr int a = u2_addr<NL>(q, line);
      if constexpr (z > 0) {
        constexpr int zp = z - 1, ap = u2_addr<NL>(zp / (2 * NL), (zp % (2 * NL)) % NL);
        if constexpr ((a >> 16) != (ap >> 16)) {
#pragma unroll
          for (int t = 0; t < 4; ++t) adj(w2a[t], ((a >> 16) - (ap >> 16)) * 65536);
        }
      }
#pragma unroll
      for (int t = 0; t < 2; ++t) A[z % 3][t] = rd_tr(w2a[2 * half + t], a & 65535, (a & 65535) + 2048);
    };
    pre(reads);
    const ldsp xw = smem + (NL == 2 ? wave * 8192 : 73728 + wave * 12288) + l16;
    const ldsp xr = smem + (NL == 2 ? (wave ^ 1) * 8192 : 73728 + (wave ^ 1) * 12288) + l16;
    sfor<0, NZ>([&](auto z_) {
      constexpr int z = z_, q = z / (2 * NL), i = z % (2 * NL), half = i / NL, line = i % NL;
      hook(z_);
      SCHED();
      if constexpr (z + 2 < NZ) {
        if constexpr ((z + 2) % (2 * NL) == 0) bar();
        reads(IC(z + 2){});
      }
#pragma unroll
      for (int t = 0; t < 2; ++t)
#pragma unroll
        for (int rb = 0; rb < 2; ++rb)
          acc[rb][line][2 * half + t] = mfma(A[z % 3][t], __builtin_bit_cast(v8bf, B[q][rb]),
                                             q == 0 ? v4f{0.f, 0.f, 0.f, 0.f} : acc[rb][line][2 * half + t]);
      if constexpr (q == 8 && half == 1) {
#pragma unroll
        for (int t = 0; t < 2; ++t)
#pragma unroll
          for (int rb = 0; rb < 2; ++rb)
            *(__attribute__((address_space(3))) v4f*)(xw + (rb * 2 * NL + line * 2 + t) * 1024) = acc[rb][line][t];
#pragma unroll
        for (int t = 0; t < 4; ++t) { SG(0x008, 1); SG(0x200, 1); }
      } else {
#pragma unroll
        for (int t = 0; t < 4; ++t) { SG(0x008, 1); SG(0x100, 1); }
      }
      SCHED();
    });
    STAMP(5);
    bar_lgkm();
    STAMP(6);
    const unsigned ptag = htag >> 15;
#pragma unroll
    for (int rb = 0; rb < 2; ++rb)
#pragma unroll
      for (int l = 0; l < NL; ++l) {
        const v4f o0 = *(__attribute__((address_space(3))) v4f*)(xr + (rb * 2 * NL + 2 * l) * 1024) + acc[rb][l][2];
        const v4f o1 = *(__attribute__((address_space(3))) v4f*)(xr + (rb * 2 * NL + 2 * l + 1) * 1024) + acc[rb][l][3];
        const v4u d = {pk_bf16(o0[0], o0[1]), pk_bf16(o0[2], o0[3]), pk_bf16(o1[0], o1[1]), pk_bf16(o1[2], o1[3])};
        st16((d & 0xfffefffeu) | ptag, rws, l16 + l * 1024, pown + rb * 3072);
      }
    SCHED();
    bar();
    STAMP(7);
  };

  if (!helper) {
    const unsigned hown = WS_H + b * 24576 + wave * 6144;
    {
      const rsrc_t rb1 = __builtin_amdgcn_make_buffer_rsrc((void*)b1, 0, FF * 2, 0x00020000);
      const int r8 = lane >> 3;
      dma_one(rb1, lds0 + BIAS1, (j * 576 + c * 192) * 2, r8 < 3 ? ((lsel >> (4 * r8)) & 3) * 128 + (lane & 7) * 16 : 0x80000000u);
    }
    const int b1i = (b & ~24) | ((c == 2 ? 0 : c + 1) << 3), b2i = (b & ~24) | ((c == 0 ? 2 : c - 1) << 3);
    const unsigned hp1 = WS_H + b1i * 24576 + wave * 6144, hp2 = WS_H + b2i * 24576 + wave * 6144;
    ldsp xa[2], wa[2];
    {
      const int sg = ((lane >> 3) & 1) | ((fg & 1) << 1);
#pragma unroll
      for (int t = 0; t < 2; ++t) {
        xa[t] = smem + (32 * rh + fn) * 128 + (((4 * t + fg) ^ (fn & 6)) * 16);
        wa[t] = smem + (8 * fg + ((lane >> 2) & 3)) * 128 + (((2 * ch + t) ^ sg) * 32) + (lane & 3) * 8;
      }
    }
    const ldsp bp = smem + BIAS1 + ch * 64 + fg * 8;
    v8bf X[9][2][2];
    v8bf A[3][2];
    v4f acc[2][2][2];
    v2u bias[2];
    v4u B[9][2];
    auto reads = [&](auto z_) {
      constexpr int z = z_, v = z >> 1, ks = z & 1, a = u1_addr(v);
      if constexpr (z > 0 && ks == 0) {
        constexpr int ap = u1_addr(v - 1);
        if constexpr ((a >> 16) != (ap >> 16)) {
#pragma unroll
          for (int t = 0; t < 2; ++t) {
            adj(wa[t], ((a >> 16) - (ap >> 16)) * 65536);
            if constexpr (v < 9) adj(xa[t], ((a >> 16) - (ap >> 16)) * 65536);
          }
        }
      }
      if constexpr (v % 9 == 0 && ks == 0) {
#pragma unroll
        for (int t = 0; t < 2; ++t) bias[t] = *(__attribute__((address_space(3))) v2u*)(bp + (v / 9) * 128 + t * 32);
      }
      if constexpr (v < 9) {
#pragma unroll
        for (int rb = 0; rb < 2; ++rb)
          X[v][rb][ks] = *(__attribute__((address_space(3))) v8bf*)(xa[ks] + ((a - 8192) & 65535) + rb * 2048);
      }
#pragma unroll
      for (int t = 0; t < 2; ++t) A[z % 3][t] = rd_tr(wa[t], (a & 65535) + ks * 4096, (a & 65535) + ks * 4096 + 512);
    };
    auto epi = [&](auto p_, auto rb_) {
      constexpr int p = p_, rb = rb_;
      float h[8];
#pragma unroll
      for (int e = 0; e < 8; ++e) { const float t = __builtin_amdgcn_fmed3f(acc[p & 1][rb][e >> 2][e & 3], 0.f, __builtin_inff()); h[e] = t * t; }
      B[p][rb] = v4u{pk_bf16(h[0], h[1]), pk_bf16(h[2], h[3]), pk_bf16(h[4], h[5]), pk_bf16(h[6], h[7])};
      st16(B[p][rb] | htag, rws, l16 + rb * 1024, hown + p * 2048);
    };
    auto mfmas = [&](auto z_) {
      constexpr int z = z_, v = z >> 1, ks = z & 1, p = v / 9, k = v % 9;
#pragma unroll
      for (int t = 0; t < 2; ++t)
#pragma unroll
        for (int rb = 0; rb < 2; ++rb)
          acc[p & 1][rb][t] = mfma(A[z % 3][t], X[k][rb][ks],
                                   (k == 0 && ks == 0) ? v4f{bf_lo(bias[t][0]), bf_hi(bias[t][0]), bf_lo(bias[t][1]), bf_hi(bias[t][1])}
                                                       : acc[p & 1][rb][t]);
    };
    wait_vm<0>();
    asm volatile("" : "+v"(want));
    htag = (__builtin_amdgcn_readfirstlane(want) & 1) ? 0x80000000u : 0x8000u;
    SCHED();
    bar();
    STAMP(1);
    reads(IC(0){});
    reads(IC(1){});
    sfor<0, 52>([&](auto z_) {
      constexpr int z = z_, v2 = (z + 2) >> 1;
      if constexpr (z == 40) {
        B[3][0] = ld16(rws, l16, hp1); B[3][1] = ld16(rws, l16 + 1024, hp1);
        B[4][0] = ld16(rws, l16, hp2); B[4][1] = ld16(rws, l16 + 1024, hp2);
      }
      if constexpr (z == 18) { STAMP(2); }
      SCHED();
      if constexpr ((z & 1) == 0 && (v2 < 9 || ((v2 - 9) & 1) == 0)) bar();
      reads(IC(z + 2){});
      mfmas(z_);
      if constexpr (z == 19 || z == 20) epi(IC(0){}, IC(z - 19){});
      if constexpr (z == 37 || z == 38) epi(IC(1){}, IC(z - 37){});
      if constexpr (z % 18 == 0) SG(0x002, 8);
#pragma unroll
      for (int t = 0; t < 4; ++t) { SG(0x008, 1); SG(0x100, z + 2 < 18 ? 2 : 1); SG(0x002, 5); }
      SCHED();
    });
    phase2(IC(2){}, B,
           [&](auto&& rd2) {
             bar();
             rd2(IC(0){});
             mfmas(IC(52){});
#pragma unroll
             for (int t = 0; t < 4; ++t) { SG(0x008, 1); SG(0x100, 1); }
             SCHED();
             rd2(IC(1){});
             mfmas(IC(53){});
#pragma unroll
             for (int t = 0; t < 4; ++t) { SG(0x008, 1); SG(0x100, 1); }
             SCHED();
             epi(IC(2){}, IC(0){});
             epi(IC(2){}, IC(1){});
             SCHED();
             STAMP(3);
           },
           [&](auto z_) {
             constexpr int z = z_;
             if constexpr (z == 8) {
               B[5][0] = ld16(rws, l16, hp1 + 2048); B[5][1] = ld16(rws, l16 + 1024, hp1 + 2048);
               B[6][0] = ld16(rws, l16, hp2 + 2048); B[6][1] = ld16(rws, l16 + 1024, hp2 + 2048);
             }
             if constexpr (z == 20) {
               B[7][0] = ld16(rws, l16, hp1 + 4096); B[7][1] = ld16(rws, l16 + 1024, hp1 + 4096);
               B[8][0] = ld16(rws, l16, hp2 + 4096); B[8][1] = ld16(rws, l16 + 1024, hp2 + 4096);
             }
             if constexpr (z % 4 == 0 && z >= 12) {
               constexpr int q = z / 4;
               if constexpr (q == 7) { STAMP(4); }
               ensure_h(B[q][0], B[q][1], htag, rws, l16, ((q & 1) ? hp1 : hp2) + ((q - 3) >> 1) * 2048);
             }
           });
  } else {
    const unsigned hp = WS_H + (b & ~24) * 24576 + wave * 6144;
    const int orow = mt * 64 + 32 * (j >> 2) + 16 * (j & 1) + fn;
    const int ocol = 384 + 128 + 32 * ((j >> 1) & 1) + 16 * (wave & 1) + 4 * fg;
    const rsrc_t ro = __builtin_amdgcn_make_buffer_rsrc((void*)out, 0, MROWS * DM * 2, 0x00020000);
    const rsrc_t rb2 = __builtin_amdgcn_make_buffer_rsrc((void*)b2, 0, DM * 2, 0x00020000);
    const v2u bb = __builtin_amdgcn_raw_buffer_load_b64(rb2, ocol * 2, 0, 0);
    v4u B[9][2];
    asm volatile("" : "+v"(want));
    htag = (__builtin_amdgcn_readfirstlane(want) & 1) ? 0x80000000u : 0x8000u;
    bar();
    STAMP(1);
    phase2(IC(3){}, B,
           [&](auto&& rd2) { rd2(IC(0){}); rd2(IC(1){}); },
           [&](auto z_) {
             constexpr int z = z_;
             if constexpr (z % 18 == 0) {
               constexpr int p = z / 18;
               if constexpr (p == 2) { STAMP(3); }
#pragma unroll
               for (int cc = 0; cc < 3; ++cc) {
                 B[3 * p + cc][0] = ld16(rws, l16, hp + cc * (8 * 24576) + p * 2048);
                 B[3 * p + cc][1] = ld16(rws, l16 + 1024, hp + cc * (8 * 24576) + p * 2048);
               }
             }
             if constexpr (z % 6 == 0) {
               constexpr int q = z / 6;
               ensure_h(B[q][0], B[q][1], htag, rws, l16, hp + (q % 3) * (8 * 24576) + (q / 3) * 2048);
               if constexpr (q == 6) { STAMP(4); }
             }
           });
    if (wave < 2) {
      __builtin_amdgcn_s_sleep(PSLEEP);
      reduce_tile(rws, ro, WS_P + (b & ~7) * 24576 + j * 3072, 2048 + l16 + wave * 8, htag >> 15, bb, orow * (DM * 2) + ocol * 2);
    }
  }
  STAMP(9);
  STAMP_OUT();
}

extern "C" void kernel_launch(void* const* d_in, const int* in_sizes, int n_in, void* d_out, int out_size,
                              void* d_ws, size_t ws_size, hipStream_t stream) {
  (void)in_sizes; (void)n_in; (void)out_size; (void)ws_size;
  hipFuncSetAttribute((const void*)mlp_fused, hipFuncAttributeMaxDynamicSharedMemorySize, LDS_BYTES);
  mlp_fused<<<256, 512, LDS_BYTES, stream>>>((const char*)d_in[0], (const char*)d_in[1], (const char*)d_in[2],
                                             (const char*)d_in[3], (const char*)d_in[4], (char*)d_out, (char*)d_ws);
}
```

```cpp
#define LAYOUT 0
#define PFSLEEP 75
#include <hip/hip_runtime.h>
#include <hip/hip_bf16.h>
#include <stdint.h>
#include <type_traits>

typedef __attribute__((ext_vector_type(4))) short v4s;
typedef __attribute__((ext_vector_type(8))) short v8s;
typedef __attribute__((ext_vector_type(8))) __bf16 v8bf;
typedef __attribute__((ext_vector_type(2))) __bf16 v2bf;
typedef __attribute__((ext_vector_type(4))) float v4f;
typedef __attribute__((ext_vector_type(2))) float v2f;
typedef __attribute__((ext_vector_type(4))) unsigned v4u;
typedef __attribute__((ext_vector_type(2))) unsigned v2u;
typedef __attribute__((address_space(3))) char* ldsp;
typedef __attribute__((address_space(1))) unsigned long long gu64;
typedef unsigned long long u64;
typedef __amdgpu_buffer_rsrc_t rsrc_t;

#define DI __device__ __forceinline__
#define IC(n) std::integral_constant<int, n>

#define DEPTH 5
#define LATEOPS 2
#define PSLEEP 2

constexpr int MROWS = 512, DM = 576, FF = 4608;
constexpr int BIAS1 = 147456;
constexpr int DUMMY = 147456 + 1024;
constexpr int LDS_BYTES = 147456 + 1024 + 256;
constexpr int WS_EM = 256, WS_MT = 32 * 24576, WS_H = 4096, WS_P = WS_H + 256 * 24576, WS_END = WS_P + 256 * 24576;
#define GRP(n) (n)
#define NTS ""
constexpr int SPIN_MAX = 100000;
#define STAMP(i)
#define STAMP_OUT()
#define WB(n) do { wait_vm<n>(); bar(); } while (0)

template <int I, int N, class F> DI void sfor(F&& f) {
  if constexpr (I < N) { f(IC(I){}); sfor<I + 1, N>(f); }
}
template <int N> DI void wait_vm() { asm volatile("s_waitcnt vmcnt(%0)" ::"n"(N) : "memory"); }
DI void bar() { asm volatile("s_barrier" ::: "memory"); }
DI void bar_lgkm() { asm volatile("s_waitcnt lgkmcnt(0)\n\ts_barrier" ::: "memory"); }

DI void dma_unit(rsrc_t rs, unsigned lds, unsigned soff, unsigned v0, unsigned v1) {
  asm volatile(
      "s_mov_b32 m0, %0\n\t"
      "s_nop 0\n\t"
      "buffer_load_dwordx4 %3, %1, %2 offen" NTS " lds\n\t"
      "buffer_load_dwordx4 %4, %1, %2 offen offset:1024" NTS " lds"
      ::"s"(lds), "s"(rs), "s"(soff), "v"(v0), "v"(v1)
      : "memory");
}
DI void dma_one(rsrc_t rs, unsigned lds, unsigned soff, unsigned v0) {
  asm volatile(
      "s_nop 4\n\t"
      "s_mov_b32 m0, %0\n\t"
      "s_nop 0\n\t"
      "buffer_load_dwordx4 %3, %1, %2 offen lds"
      ::"s"(lds), "s"(rs), "s"(soff), "v"(v0)
      : "memory");
}
DI void touch(rsrc_t rs, unsigned lds, unsigned soff, unsigned v0) {
  asm volatile(
      "s_nop 4\n\t"
      "s_mov_b32 m0, %0\n\t"
      "s_nop 0\n\t"
      "buffer_load_dword %3, %1, %2 offen lds"
      ::"s"(lds), "s"(rs), "s"(soff), "v"(v0)
      : "memory");
}
DI v8bf rd_tr(ldsp p, int lo, int hi) {
  v4s a = __builtin_amdgcn_ds_read_tr16_b64_v4i16((__attribute__((address_space(3))) v4s*)(p + lo));
  v4s b = __builtin_amdgcn_ds_read_tr16_b64_v4i16((__attribute__((address_space(3))) v4s*)(p + hi));
  v8s ab = __builtin_shufflevector(a, b, 0, 1, 2, 3, 4, 5, 6, 7);
  return __builtin_bit_cast(v8bf, ab);
}
DI unsigned pk_bf16(float a, float b) {
  v2f f = {a, b};
  return __builtin_bit_cast(unsigned, __builtin_convertvector(f, v2bf));
}
DI float bf_lo(unsigned u) { return __builtin_bit_cast(float, u << 16); }
DI float bf_hi(unsigned u) { return __builtin_bit_cast(float, u & 0xffff0000u); }
DI v4f mfma(v8bf a, v8bf b, v4f c) {
  return __builtin_amdgcn_mfma_f32_16x16x32_bf16(a, b, c, 0, 0, 0); }
DI v4u ld16(rsrc_t r, unsigned v, unsigned s) { return __builtin_amdgcn_raw_buffer_load_b128(r, v, s, 16); }
DI void st16(v4u d, rsrc_t r, unsigned v, unsigned s) { __builtin_amdgcn_raw_buffer_store_b128(d, r, v, s, 16); }
typedef __attribute__((address_space(1))) unsigned gu32;
DI unsigned ld_flag(const char* p) { return __hip_atomic_load((gu32*)p, __ATOMIC_RELAXED, __HIP_MEMORY_SCOPE_AGENT); }
DI void st_flag(char* p, unsigned v) { __hip_atomic_store((gu32*)p, v, __ATOMIC_RELAXED, __HIP_MEMORY_SCOPE_AGENT); }
DI void adj(ldsp& p, int d) { p += d; asm volatile("" : "+v"(p)); }

#define SCHED() __builtin_amdgcn_sched_barrier(0)
#define SG(m, n) __builtin_amdgcn_sched_group_barrier(m, n, 0)

DI bool h_ok(const v4u& b0, const v4u& b1, unsigned tag) {
  const v4u x0 = b0 ^ tag, x1 = b1 ^ tag;
  const unsigned o = (x0[0] | x0[1] | x0[2]) | (x0[3] | x1[0] | x1[1]) | (x1[2] | x1[3]);
  return __all((o & 0x80008000u) == 0);
}
template <int N> DI void ensure_h(v4u (*B)[2], unsigned tag, rsrc_t rws, unsigned v, unsigned s0, unsigned s1, unsigned s2) {
  if (__builtin_expect(!h_ok(B[0][0], B[0][1], tag), 0)) {
#pragma nounroll
    for (int sp = 0; sp < SPIN_MAX; ++sp) {
      asm volatile("" ::: "memory");
      B[0][0] = ld16(rws, v, s0);
      B[0][1] = ld16(rws, v + 1024, s0);
      if constexpr (N > 1) { B[1][0] = ld16(rws, v, s1); B[1][1] = ld16(rws, v + 1024, s1); }
      if constexpr (N > 2) { B[2][0] = ld16(rws, v, s2); B[2][1] = ld16(rws, v + 1024, s2); }
      if (h_ok(B[0][0], B[0][1], tag)) break;
    }
  }
  B[0][0] ^= tag;
  B[0][1] ^= tag;
}

constexpr int u1_addr(int v) { return v < 9 ? v * 16384 + 8192 : ((v - 9) >> 1) * 16384 + ((v - 9) & 1) * 8192; }
template <int NL> constexpr int u2_addr(int q, int line) {
  return NL == 2 ? q * 16384 + line * 8192 : (q % 6) * 24576 + line * 8192;
}

DI void reduce_tile(rsrc_t rws, rsrc_t ro, unsigned pbase, unsigned voff, unsigned ptag, v2u bb, unsigned ooff) {
  v4u p[4];
#pragma nounroll
  for (int sp = 0; sp < SPIN_MAX; ++sp) {
    asm volatile("" ::: "memory");
#pragma unroll
    for (int jj = 0; jj < 4; ++jj) p[jj] = ld16(rws, voff, pbase + jj * 24576);
    unsigned m = 0;
#pragma unroll
    for (int jj = 0; jj < 4; ++jj) { p[jj] ^= ptag; m |= (p[jj][0] | p[jj][1]) | (p[jj][2] | p[jj][3]); }
    if (__all((m & 0x00010001u) == 0)) break;
  }
  v4f s0 = {0.f, 0.f, 0.f, 0.f}, s1 = s0;
#pragma unroll
  for (int jj = 0; jj < 4; ++jj) {
    const v4u u0 = {(p[jj][0] << 16) | 0x8000u, (p[jj][0] & 0xffff0000u) | 0x8000u,
                    (p[jj][1] << 16) | 0x8000u, (p[jj][1] & 0xffff0000u) | 0x8000u};
    const v4u u1 = {(p[jj][2] << 16) | 0x8000u, (p[jj][2] & 0xffff0000u) | 0x8000u,
                    (p[jj][3] << 16) | 0x8000u, (p[jj][3] & 0xffff0000u) | 0x8000u};
    s0 = jj ? s0 + __builtin_bit_cast(v4f, u0) : __builtin_bit_cast(v4f, u0);
    s1 = jj ? s1 + __builtin_bit_cast(v4f, u1) : __builtin_bit_cast(v4f, u1);
  }
  v4f s = {bf_lo(bb[0]), bf_hi(bb[0]), bf_lo(bb[1]), bf_hi(bb[1])};
#pragma unroll
  for (int i = 0; i < 4; ++i) {
    const auto r = __builtin_amdgcn_permlane32_swap(__float_as_uint(s0[i]), __float_as_uint(s1[i]), false, false);
    s[i] += __uint_as_float(r[0]) + __uint_as_float(r[1]);
  }
  const v2u o = {pk_bf16(s[0], s[1]), pk_bf16(s[2], s[3])};
  __builtin_amdgcn_raw_buffer_store_b64(o, ro, ooff, 0, 0);
}

__global__ __launch_bounds__(512, 2) void mlp_fused(const char* __restrict__ x, const char* __restrict__ w1,
                                                    const char* __restrict__ b1, const char* __restrict__ w2,
                                                    const char* __restrict__ b2, char* __restrict__ out,
                                                    char* __restrict__ ws) {
  extern __shared__ __attribute__((aligned(16))) char smem_[];
  const ldsp smem = (ldsp)smem_;
  const unsigned lds0 = (unsigned)(uintptr_t)smem;
  const int b = blockIdx.x, j = b & 7, mt = b >> 5, c = (b >> 3) & 3;
  const int lane = threadIdx.x & 63;
  const int wave = __builtin_amdgcn_readfirstlane(threadIdx.x >> 6);
  STAMP(0);
  u64 pc0;
  asm volatile("s_getpc_b64 %0\n.Lmlp_pc:" : "=s"(pc0));
  asm volatile("" ::"s"(x), "s"(w1), "s"(b1), "s"(w2), "s"(b2), "s"(out), "s"(ws));
  char* const epoch_p = ws + mt * WS_EM;
  const unsigned lsel = (unsigned)(0x210210210210ull >> (4 * mt));
  const unsigned l0 = lsel & 3, l1 = (lsel >> 4) & 3, l2 = (lsel >> 8) & 3;
  const bool helper = c == 3;
  unsigned want;

  if (__builtin_expect(wave >= 4, 1)) {
    const int lw = wave - 4, r8 = lane >> 3, p16 = lane & 7;
    const rsrc_t rw2 = __builtin_amdgcn_make_buffer_rsrc((void*)w2, 0, DM * FF * 2, 0x00020000);
    const unsigned mb = lds0 + lw * 2048;
    const unsigned v20 = (16 * lw + r8) * (DM * 2) + ((p16 ^ (r8 & 6)) * 16);
    const unsigned v21 = v20 + 8 * DM * 2 - 1024;
    const unsigned w2s = j * (576 * DM * 2) + c * 256;
    v2u bb;
    int lane2 = lane;
    auto late = [&]() {
      asm volatile("" : "+v"(lane2));
      const rsrc_t rb2 = __builtin_amdgcn_make_buffer_rsrc((void*)b2, 0, DM * 2, 0x00020000);
      bb = __builtin_amdgcn_raw_buffer_load_b64(
          rb2, (128 * c + 64 * (lw >> 1) + 32 * ((j >> 1) & 1) + 8 * (lw & 1) + 16 * (lane2 >> 5) + 4 * ((lane2 >> 4) & 1)) * 2, 0, 0);
      touch(__builtin_amdgcn_make_buffer_rsrc((void*)out, 0, MROWS * DM * 2, 0x00020000), lds0 + DUMMY, mt * 64 * DM * 2,
            lane2 == 0 ? 0u : 0x80000000u);
      SCHED();
    };
    if (!helper) {
      const rsrc_t rx = __builtin_amdgcn_make_buffer_rsrc((void*)x, 0, MROWS * DM * 2, 0x00020000);
      const rsrc_t rw1 = __builtin_amdgcn_make_buffer_rsrc((void*)w1, 0, DM * FF * 2, 0x00020000);
      const unsigned cb = (j * 576 + c * 192) * 2;
      const unsigned v10 = (16 * lw + r8) * (FF * 2) + ((p16 ^ (r8 & 2)) * 16);
      const unsigned v11 = (v10 ^ 64) + 8 * FF * 2 - 1024;
      unsigned ko = 3 * ((0x10210u >> (4 * (c + ((mt * 11) >> 5)))) & 3);
      const unsigned xs = mt * 64 * DM * 2;
      asm volatile("s_nop 4");
#pragma nounroll
      for (int t = 0; t < 9; ++t) {
        if (t >= DEPTH) { wait_vm<4 * (DEPTH - 1)>(); bar(); }
        dma_unit(rx, mb + t * 16384, xs + ko * 128, v20, v21);
        dma_unit(rw1, mb + t * 16384 + 8192, ko * (64 * FF * 2) + cb + l0 * 128, v10, v11);
        ko = ko == 8 ? 0 : ko + 1;
        if (t == 0) want = ld_flag(epoch_p);
      }
      STAMP(1);
#pragma nounroll
      for (int t = 0; t < 9; ++t) {
        WB(4 * (DEPTH - 1));
        dma_unit(rw1, mb + t * 16384, ko * (64 * FF * 2) + cb + (t < 5 ? l1 : l2) * 128, v10, v11);
        ko = ko == 8 ? 0 : ko + 1;
        dma_unit(rw1, mb + t * 16384 + 8192, ko * (64 * FF * 2) + cb + (t < 4 ? l1 : l2) * 128, v10, v11);
        ko = ko == 8 ? 0 : ko + 1;
      }
      STAMP(2);
      const unsigned csel = 0x10210u >> (4 * c), a0 = 3 * c, a1 = 3 * ((csel >> 4) & 3), a2 = 3 * ((csel >> 8) & 3);
      u64 tab = (l0 * 0x00011001u + l1 * 0x01100010u + l2 * 0x10000100u + a0 * 0x111u + a1 * 0x10101000u + a2 * 0x01010000u) |
                ((u64)(l2 + a2) << 32);
#pragma nounroll
      for (int q = 0; q < 9; ++q) {
        const unsigned so = w2s + ((unsigned)tab & 15) * (64 * DM * 2);
        tab >>= 4;
        WB(4 * (DEPTH - 1));
        dma_unit(rw2, mb + q * 16384, so, v20, v21);
        dma_unit(rw2, mb + q * 16384 + 8192, so + 128, v20, v21);
      }
      STAMP(3);
      late();
      sfor<0, DEPTH>([&](auto r) { wait_vm<4 * (DEPTH - 1 - r) + LATEOPS>(); bar(); });
    } else {
      u64 tab = (l0 * 0x111u + l1 * 0x111000u + l2 * 0x11000000u + 0x30630630u) | ((u64)(l2 + 6) << 32);
      auto issue = [&](int slot) {
        const unsigned so = w2s + ((unsigned)tab & 15) * (64 * DM * 2);
        tab >>= 4;
        dma_unit(rw2, mb + slot * 24576, so, v20, v21);
        dma_unit(rw2, mb + slot * 24576 + 8192, so + 128, v20, v21);
        dma_unit(rw2, mb + slot * 24576 + 16384, so + 256, v20, v21);
      };
      want = ld_flag(epoch_p);
      asm volatile("s_nop 4");
#pragma nounroll
      for (int q = 0; q < 6; ++q) issue(q);
      STAMP(1);
      wait_vm<30>(); bar();
      wait_vm<24>(); bar();
#pragma nounroll
      for (int q = 6; q < 9; ++q) { wait_vm<18>(); bar(); issue(q - 6); }
      STAMP(3);
      late();
      wait_vm<18 + LATEOPS>(); bar();
      wait_vm<12 + LATEOPS>(); bar();
      wait_vm<6 + LATEOPS>(); bar();
      wait_vm<LATEOPS>(); bar();
    }
    const rsrc_t rws = __builtin_amdgcn_make_buffer_rsrc((void*)ws, 0, WS_END, 0x00020000);
    const rsrc_t ro = __builtin_amdgcn_make_buffer_rsrc((void*)out, 0, MROWS * DM * 2, 0x00020000);
    const int orow = mt * 64 + 32 * (j >> 2) + 16 * (j & 1) + (lane2 & 15);
    const int ocol = 128 * c + 64 * (lw >> 1) + 32 * ((j >> 1) & 1) + 8 * (lw & 1) + 16 * (lane2 >> 5) + 4 * ((lane2 >> 4) & 1);
    STAMP(4);
    asm volatile("" : "+v"(want));
    want = __builtin_amdgcn_readfirstlane(want) + 1;
    bar();
    STAMP(6);
    bar();
    __builtin_amdgcn_s_sleep(PSLEEP);
    STAMP(7);
    reduce_tile(rws, ro, mt * WS_MT + WS_P + (b & 24) * 24576 + j * 3072, lw * 512 + (lane2 & 31) * 16 + (lane2 >> 5) * (4 * 24576),
                (want & 1) ? 1u : 0x10000u, bb, orow * (DM * 2) + ocol * 2);
    if (b == (mt * 32 + 24) && threadIdx.x == 256) st_flag(epoch_p, want);
    STAMP(9);
    STAMP_OUT();
    return;
  }

  want = ld_flag(epoch_p);
  const rsrc_t rws = __builtin_amdgcn_make_buffer_rsrc((void*)ws, 0, WS_END, 0x00020000);
  const int fn = lane & 15, fg = lane >> 4;
  const int rh = wave >> 1, ch = wave & 1;
  const unsigned l16 = lane * 16;
  const unsigned pown = mt * WS_MT + WS_P + (b & 31) * 24576 + wave * 6144;
  unsigned htag;
  ldsp w2a[4];
#pragma unroll
  for (int t = 0; t < 4; ++t)
    w2a[t] = smem + ch * 4096 + (lane >> 2) * 128 + ((t ^ (2 * (ch ^ 1)) ^ ((lane >> 3) & 3)) * 32) + (lane & 3) * 8;

  auto phase2 = [&](auto nl, v4u (&B)[9][2], auto&& pre, auto&& hook) {
    constexpr int NL = nl;
    constexpr int NZ = 9 * 2 * NL;
    v4f acc[2][NL][4];
    v8bf A[3][2];
    auto reads = [&](auto z_) {
      constexpr int z = z_, q = z / (2 * NL), i = z % (2 * NL), half = i / NL, line = i % NL;
      constexpr int a = u2_addr<NL>(q, line);
      if constexpr (z > 0) {
        constexpr int zp = z - 1, ap = u2_addr<NL>(zp / (2 * NL), (zp % (2 * NL)) % NL);
        if constexpr ((a >> 16) != (ap >> 16)) {
#pragma unroll
          for (int t = 0; t < 4; ++t) adj(w2a[t], ((a >> 16) - (ap >> 16)) * 65536);
        }
      }
#pragma unroll
      for (int t = 0; t < 2; ++t) A[z % 3][t] = rd_tr(w2a[2 * half + t], a & 65535, (a & 65535) + 2048);
    };
    pre(reads);
    const ldsp xw = smem + (NL == 2 ? wave * 8192 : 73728 + wave * 12288) + l16;
    const ldsp xr = smem + (NL == 2 ? (wave ^ 1) * 8192 : 73728 + (wave ^ 1) * 12288) + l16;
    sfor<0, NZ>([&](auto z_) {
      constexpr int z = z_, q = z / (2 * NL), i = z % (2 * NL), half = i / NL, line = i % NL;
      hook(z_);
      SCHED();
      if constexpr (z + 2 < NZ) {
        if constexpr ((z + 2) % (2 * NL) == 0) bar();
        reads(IC(z + 2){});
      }
#pragma unroll
      for (int t = 0; t < 2; ++t)
#pragma unroll
        for (int rb = 0; rb < 2; ++rb)
          acc[rb][line][2 * half + t] = mfma(A[z % 3][t], __builtin_bit_cast(v8bf, B[q][rb]),
                                             q == 0 ? v4f{0.f, 0.f, 0.f, 0.f} : acc[rb][line][2 * half + t]);
      if constexpr (q == 8 && half == 1) {
#pragma unroll
        for (int t = 0; t < 2; ++t)
#pragma unroll
          for (int rb = 0; rb < 2; ++rb)
            *(__attribute__((address_space(3))) v4f*)(xw + (rb * 2 * NL + line * 2 + t) * 1024) = acc[rb][line][t];
#pragma unroll
        for (int t = 0; t < 4; ++t) { SG(0x008, 1); SG(0x200, 1); }
      } else {
#pragma unroll
        for (int t = 0; t < 4; ++t) { SG(0x008, 1); SG(0x100, 1); }
      }
      SCHED();
    });
    STAMP(5);
    bar_lgkm();
    STAMP(6);
    const unsigned ptag = htag >> 15;
#pragma unroll
    for (int rb = 0; rb < 2; ++rb)
#pragma unroll
      for (int l = 0; l < NL; ++l) {
        const v4f o0 = *(__attribute__((address_space(3))) v4f*)(xr + (rb * 2 * NL + 2 * l) * 1024) + acc[rb][l][2];
        const v4f o1 = *(__attribute__((address_space(3))) v4f*)(xr + (rb * 2 * NL + 2 * l + 1) * 1024) + acc[rb][l][3];
        const v4u d = {pk_bf16(o0[0], o0[1]), pk_bf16(o0[2], o0[3]), pk_bf16(o1[0], o1[1]), pk_bf16(o1[2], o1[3])};
        st16((d & 0xfffefffeu) | ptag, rws, l16 + l * 1024, pown + rb * 3072);
      }
    SCHED();
    bar();
    STAMP(7);
  };

  if (!helper) {
    const unsigned hown = mt * WS_MT + WS_H + (b & 31) * 24576 + wave * 6144;
    {
      const rsrc_t rb1 = __builtin_amdgcn_make_buffer_rsrc((void*)b1, 0, FF * 2, 0x00020000);
      const int r8 = lane >> 3;
      dma_one(rb1, lds0 + BIAS1, (j * 576 + c * 192) * 2, r8 < 3 ? ((lsel >> (4 * r8)) & 3) * 128 + (lane & 7) * 16 : 0x80000000u);
    }
    const int b1i = j | ((c == 2 ? 0 : c + 1) << 3), b2i = j | ((c == 0 ? 2 : c - 1) << 3);
    const unsigned hp1 = mt * WS_MT + WS_H + b1i * 24576 + wave * 6144, hp2 = mt * WS_MT + WS_H + b2i * 24576 + wave * 6144;
    ldsp xa[2], wa[2];
    {
      const int sg = ((lane >> 3) & 1) | ((fg & 1) << 1);
#pragma unroll
      for (int t = 0; t < 2; ++t) {
        xa[t] = smem + (32 * rh + fn) * 128 + (((4 * t + fg) ^ (fn & 6)) * 16);
        wa[t] = smem + (8 * fg + ((lane >> 2) & 3)) * 128 + (((2 * ch + t) ^ sg) * 32) + (lane & 3) * 8;
      }
    }
    const ldsp bp = smem + BIAS1 + ch * 64 + fg * 8;
    v8bf X[9][2][2];
    v8bf A[3][2];
    v4f acc[2][2][2];
    v2u bias[2];
    v4u B[9][2];
    auto reads = [&](auto z_) {
      constexpr int z = z_, v = z >> 1, ks = z & 1, a = u1_addr(v);
      if constexpr (z > 0 && ks == 0) {
        constexpr int ap = u1_addr(v - 1);
        if constexpr ((a >> 16) != (ap >> 16)) {
#pragma unroll
          for (int t = 0; t < 2; ++t) {
            adj(wa[t], ((a >> 16) - (ap >> 16)) * 65536);
            if constexpr (v < 9) adj(xa[t], ((a >> 16) - (ap >> 16)) * 65536);
          }
        }
      }
      if constexpr (v % 9 == 0 && ks == 0) {
#pragma unroll
        for (int t = 0; t < 2; ++t) bias[t] = *(__attribute__((address_space(3))) v2u*)(bp + (v / 9) * 128 + t * 32);
      }
      if constexpr (v < 9) {
#pragma unroll
        for (int rb = 0; rb < 2; ++rb)
          X[v][rb][ks] = *(__attribute__((address_space(3))) v8bf*)(xa[ks] + ((a - 8192) & 65535) + rb * 2048);
      }
#pragma unroll
      for (int t = 0; t < 2; ++t) A[z % 3][t] = rd_tr(wa[t], (a & 65535) + ks * 4096, (a & 65535) + ks * 4096 + 512);
    };
    auto epi = [&](auto p_, auto rb_) {
      constexpr int p = p_, rb = rb_;
      float h[8];
#pragma unroll
      for (int e = 0; e < 8; ++e) { const float t = __builtin_amdgcn_fmed3f(acc[p & 1][rb][e >> 2][e & 3], 0.f, __builtin_inff()); h[e] = t * t; }
      B[p][rb] = v4u{pk_bf16(h[0], h[1]), pk_bf16(h[2], h[3]), pk_bf16(h[4], h[5]), pk_bf16(h[6], h[7])};
      st16(B[p][rb] | htag, rws, l16 + rb * 1024, hown + p * 2048);
    };
    auto mfmas = [&](auto z_) {
      constexpr int z = z_, v = z >> 1, ks = z & 1, p = v / 9, k = v % 9;
#pragma unroll
      for (int t = 0; t < 2; ++t)
#pragma unroll
        for (int rb = 0; rb < 2; ++rb)
          acc[p & 1][rb][t] = mfma(A[z % 3][t], X[k][rb][ks],
                                   (k == 0 && ks == 0) ? v4f{bf_lo(bias[t][0]), bf_hi(bias[t][0]), bf_lo(bias[t][1]), bf_hi(bias[t][1])}
                                                       : acc[p & 1][rb][t]);
    };
    wait_vm<0>();
    asm volatile("" : "+v"(want));
    htag = (__builtin_amdgcn_readfirstlane(want) & 1) ? 0x80000000u : 0x8000u;
    SCHED();
    bar();
    STAMP(1);
    reads(IC(0){});
    reads(IC(1){});
    sfor<0, 52>([&](auto z_) {
      constexpr int z = z_, v2 = (z + 2) >> 1;
      if constexpr (z == 40) {
        B[3][0] = ld16(rws, l16, hp1); B[3][1] = ld16(rws, l16 + 1024, hp1);
        B[4][0] = ld16(rws, l16, hp2); B[4][1] = ld16(rws, l16 + 1024, hp2);
      }
      if constexpr (z == 18) { STAMP(2); }
      SCHED();
      if constexpr ((z & 1) == 0 && (v2 < 9 || ((v2 - 9) & 1) == 0)) bar();
      reads(IC(z + 2){});
      mfmas(z_);
      if constexpr (z == 19 || z == 20) epi(IC(0){}, IC(z - 19){});
      if constexpr (z == 37 || z == 38) epi(IC(1){}, IC(z - 37){});
      if constexpr (z % 18 == 0) SG(0x002, 8);
#pragma unroll
      for (int t = 0; t < 4; ++t) { SG(0x008, 1); SG(0x100, z + 2 < 18 ? 2 : 1); SG(0x002, 5); }
      SCHED();
    });
    phase2(IC(2){}, B,
           [&](auto&& rd2) {
             bar();
             rd2(IC(0){});
             mfmas(IC(52){});
#pragma unroll
             for (int t = 0; t < 4; ++t) { SG(0x008, 1); SG(0x100, 1); }
             SCHED();
             rd2(IC(1){});
             mfmas(IC(53){});
#pragma unroll
             for (int t = 0; t < 4; ++t) { SG(0x008, 1); SG(0x100, 1); }
             SCHED();
             epi(IC(2){}, IC(0){});
             epi(IC(2){}, IC(1){});
             SCHED();
             STAMP(3);
           },
           [&](auto z_) {
             constexpr int z = z_;
             if constexpr (z == 8) {
               B[5][0] = ld16(rws, l16, hp1 + 2048); B[5][1] = ld16(rws, l16 + 1024, hp1 + 2048);
               B[6][0] = ld16(rws, l16, hp2 + 2048); B[6][1] = ld16(rws, l16 + 1024, hp2 + 2048);
             }
             if constexpr (z == 20) {
               B[7][0] = ld16(rws, l16, hp1 + 4096); B[7][1] = ld16(rws, l16 + 1024, hp1 + 4096);
               B[8][0] = ld16(rws, l16, hp2 + 4096); B[8][1] = ld16(rws, l16 + 1024, hp2 + 4096);
             }
             if constexpr (z % 4 == 0 && z >= 12) {
               constexpr int q = z / 4;
               if constexpr (q == 7) { STAMP(4); }
               ensure_h<GRP((q & 1) ? 2 : 1)>(&B[q], htag, rws, l16, ((q & 1) ? hp1 : hp2) + ((q - 3) >> 1) * 2048, hp2 + ((q - 3) >> 1) * 2048, 0);
             }
           });
  } else {
    const unsigned hp = mt * WS_MT + WS_H + j * 24576 + wave * 6144;
    {
      unsigned len;
      asm volatile("s_mov_b32 %0, .Lmlp_end-.Lmlp_pc" : "=s"(len));
      touch(__builtin_amdgcn_make_buffer_rsrc((void*)pc0, 0, len, 0x00020000), lds0 + DUMMY, wave * 8192, lane * 128);
    }
    {
      const rsrc_t rw2 = __builtin_amdgcn_make_buffer_rsrc((void*)w2, 0, DM * FF * 2, 0x00020000);
      __builtin_amdgcn_s_sleep(PFSLEEP);
      const unsigned so = (j * 576 + mt * 72 + wave * 18) * (DM * 2);
      const int n1 = lane + 64;
      touch(rw2, lds0 + DUMMY, so, (lane / 6) * (DM * 2) + (lane % 6) * 128);
      touch(rw2, lds0 + DUMMY, so, n1 < 108 ? (n1 / 6) * (DM * 2) + (n1 % 6) * 128 : 0x80000000u);
    }
    const int orow = mt * 64 + 32 * (j >> 2) + 16 * (j & 1) + fn;
    const int ocol = 384 + 128 + 32 * ((j >> 1) & 1) + 8 * (wave & 1) + 16 * (lane >> 5) + 4 * (fg & 1);
    const rsrc_t ro = __builtin_amdgcn_make_buffer_rsrc((void*)out, 0, MROWS * DM * 2, 0x00020000);
    const rsrc_t rb2 = __builtin_amdgcn_make_buffer_rsrc((void*)b2, 0, DM * 2, 0x00020000);
    const v2u bb = __builtin_amdgcn_raw_buffer_load_b64(rb2, ocol * 2, 0, 0);
    v4u B[9][2];
    asm volatile("" : "+v"(want));
    htag = (__builtin_amdgcn_readfirstlane(want) & 1) ? 0x80000000u : 0x8000u;
    bar();
    STAMP(1);
    phase2(IC(3){}, B,
           [&](auto&& rd2) { rd2(IC(0){}); rd2(IC(1){}); },
           [&](auto z_) {
             constexpr int z = z_;
             if constexpr (z % 18 == 0) {
               constexpr int p = z / 18;
               if constexpr (p == 2) { STAMP(3); }
#pragma unroll
               for (int cc = 0; cc < 3; ++cc) {
                 B[3 * p + cc][0] = ld16(rws, l16, hp + cc * (8 * 24576) + p * 2048);
                 B[3 * p + cc][1] = ld16(rws, l16 + 1024, hp + cc * (8 * 24576) + p * 2048);
               }
             }
             if constexpr (z % 6 == 0) {
               constexpr int q = z / 6;
               constexpr int o = (q % 3) * (8 * 24576) + (q / 3) * 2048;
               ensure_h<GRP(3 - q % 3)>(&B[q], htag, rws, l16, hp + o, hp + o + 8 * 24576, hp + o + 16 * 24576);
               if constexpr (q == 6) { STAMP(4); }
             }
           });
    if (wave < 2) {
      __builtin_amdgcn_s_sleep(PSLEEP);
      reduce_tile(rws, ro, mt * WS_MT + WS_P + (b & 24) * 24576 + j * 3072, 2048 + wave * 512 + (lane & 31) * 16 + (lane >> 5) * (4 * 24576), htag >> 15, bb, orow * (DM * 2) + ocol * 2);
    }
    asm volatile(".Lmlp_end:");
  }
  STAMP(9);
  STAMP_OUT();
}

extern "C" void kernel_launch(void* const* d_in, const int* in_sizes, int n_in, void* d_out, int out_size,
                              void* d_ws, size_t ws_size, hipStream_t stream) {
  (void)in_sizes; (void)n_in; (void)out_size;
  hipFuncSetAttribute((const void*)mlp_fused, hipFuncAttributeMaxDynamicSharedMemorySize, LDS_BYTES);
  mlp_fused<<<256, 512, LDS_BYTES, stream>>>((const char*)d_in[0], (const char*)d_in[1], (const char*)d_in[2],
                                             (const char*)d_in[3], (const char*)d_in[4], (char*)d_out, (char*)d_ws + ((ws_size - (size_t)WS_END) & ~(size_t)0x1FFFFF));
}
```
